```python
import torch

def _softmax(x, axis=-1):
    return torch.softmax(x, dim=axis)
def _gelu(x):
    return torch.nn.functional.gelu(x, approximate="tanh")
def _silu(x):
    return torch.nn.functional.silu(x)
def _sigmoid(x):
    return torch.sigmoid(x)
def _layernorm(x, axis=-1, eps=1e-5):
    m = x.mean(dim=axis, keepdim=True)
    v = x.var(dim=axis, keepdim=True, unbiased=False)
    return (x - m) / torch.sqrt(v + eps)
def _scatter_add(tgt, idx, src, axis=0):
    return tgt.clone().index_add_(axis, idx, src)
def _segment_sum(src, seg, n):
    out = torch.zeros((n,) + src.shape[1:], dtype=src.dtype)
    return out.index_add_(0, seg, src)
def _pad(x, pw):
    flat = tuple(v for pair in reversed(pw) for v in pair)
    return torch.nn.functional.pad(x, flat)

kernel_name = 'standalone__standard_2d_1d_3d_conv__vision'

def setup_inputs():

    _w = (lambda a: (lambda t: t.to(torch.bfloat16) if t.is_floating_point() else t)(torch.as_tensor(a)))
    H = W = 256
    C = 384
    K = 1
    return {
        "x": _w((lambda *s: torch.randn(*s, dtype=torch.bfloat16) * 0.05)(1, H, W, C)),
        "w": _w((lambda *s: torch.randn(*s, dtype=torch.bfloat16) * 0.05)(K, K, C, C)),
        "b": _w((lambda *s: torch.randn(*s, dtype=torch.bfloat16) * 0.05)(C)),
    }

def reference(x, w, b):
    B, H, W, Cin = x.shape
    K = w.shape[0]
    pl = (K - 1) // 2
    pr = K // 2
    xp = _pad(x, ((0, 0), (pl, pr), (pl, pr), (0, 0)))
    out = sum(
        torch.einsum('bhwc,cd->bhwd', xp[:, i:i+H, j:j+W, :], w[i, j])
        for i in range(K) for j in range(K)
    )
    return out + b[None, None, None, :]
```

```cpp
#include <hip/hip_runtime.h>
#include <hip/hip_bf16.h>
#include <stdint.h>


typedef __attribute__((ext_vector_type(8))) __bf16 bf16x8;
typedef __attribute__((ext_vector_type(2))) __bf16 bf16x2;
typedef __attribute__((ext_vector_type(4))) float f32x4;
typedef __attribute__((ext_vector_type(2))) float f32x2;
typedef __attribute__((ext_vector_type(4))) unsigned u32x4;

constexpr int CDIM = 384;
constexpr int ROWB = CDIM * 2;
constexpr int BM = 16;
constexpr int TILE_B = BM * ROWB;
constexpr int NWAVE = 12;
constexpr int RING = 8;
constexpr int NOB = 5;
constexpr int OROW = ROWB + 16;
constexpr int OBUF_B = BM * OROW;
constexpr int OB0 = RING * TILE_B;
constexpr int LDS_B = OB0 + NOB * OBUF_B;
constexpr int NWG = 256;
constexpr int NT = 16;
constexpr size_t TSTRIDE = (size_t)NWG * TILE_B;
constexpr int G1 = 4;
constexpr int WI = 6;
constexpr int WPS = 1;
constexpr int P1N = 4;
constexpr int P1S = 2;
constexpr int NPAIR = (NT - G1) / 2;
constexpr int BW = 0;

__device__ __forceinline__ unsigned cvt_pk(float a, float b) {
    bf16x2 r = __builtin_convertvector((f32x2){a, b}, bf16x2);
    return __builtin_bit_cast(unsigned, r);
}

struct Ctx {
    bf16x8 wf[2][12];
    unsigned d[12][8];
    u32x4 bb;
    f32x4 binit[2];
    f32x4 acc[G1][2];
    u32x4 xf[4];
    u32x4 ov[3];
    const char* xsrc;
    const char* wp;
    const char* bp;
    char* ost;
    unsigned rd[4];
    unsigned owr, ord;
    unsigned dmadst;
};

template <int N> __device__ __forceinline__ void wait_vm() {
    static_assert(N >= 0 && N < 64, "vmcnt range");
    asm volatile("s_waitcnt vmcnt(%0)" ::"n"(N) : "memory");
}
__device__ __forceinline__ void dma_rt(const char* g, unsigned dst) {
    unsigned keep;
    asm volatile("s_mov_b32 %0, m0\n\ts_mov_b32 m0, %2\n\ts_nop 0\n\tglobal_load_lds_dwordx4 %1, off\n\ts_mov_b32 m0, %0"
                 : "=&s"(keep) : "v"(g), "s"(dst) : "memory");
}
template <int T> __device__ __forceinline__ void dma_tile(const Ctx& c) {
    dma_rt(c.xsrc + (size_t)T * TSTRIDE, c.dmadst + (T % RING) * TILE_B);
}
template <int T, int E> __device__ __forceinline__ void dma_range(const Ctx& c) {
    if constexpr (T < E) { dma_tile<T>(c); dma_range<T + 1, E>(c); }
}
template <int OFF> __device__ __forceinline__ void lds_rd(u32x4& d, unsigned addr) {
    asm volatile("ds_read_b128 %0, %1 offset:%2" : "=v"(d) : "v"(addr), "n"(OFF));
}
template <int OFF> __device__ __forceinline__ void lds_wr(unsigned addr, u32x4 d) {
    asm volatile("ds_write_b128 %0, %1 offset:%2" ::"v"(addr), "v"(d), "n"(OFF) : "memory");
}
template <int N> __device__ __forceinline__ void wait_lgkm(u32x4& d) {
    asm volatile("s_waitcnt lgkmcnt(%1)" : "+v"(d) : "n"(N));
}
template <int OFF> __device__ __forceinline__ void gload1(unsigned& d, const char* p) {
    asm volatile("global_load_dword %0, %1, off offset:%2" : "=v"(d) : "v"(p), "n"(OFF));
}

constexpr int cmin(int a, int b) { return a < b ? a : b; }
constexpr int w_step(int s) { return s < WI ? -1 : (s - WI) / WPS; }
constexpr int P1O = 0;
constexpr int x1_step(int u) { return (u >= G1 && u < G1 + P1N) ? P1O + P1S * (u - G1) : -1; }
constexpr int x2_tile(int u) { return u >= G1 + P1N ? u - P1N : -1; }
enum { OP_X = 1000, OP_W = 2000, OP_ST = 3000, MK_W = 4000, MK_T = 5000 };
struct Seq { int v[600]; int n; };
constexpr Seq build_seq() {
    Seq q{}; q.n = 0;
    for (int u = 0; u < G1; ++u) q.v[q.n++] = OP_X + u;
    for (int s = 0; s < WI; ++s) { for (int j = 0; j < 8; ++j) q.v[q.n++] = OP_W + s; if (s == BW) q.v[q.n++] = OP_W + s; }
    for (int s = 0; s < 12; ++s) {
        q.v[q.n++] = MK_W + s;
        for (int v = 0; v < 12; ++v) if (w_step(v) == s) { for (int j = 0; j < 8; ++j) q.v[q.n++] = OP_W + v; if (v == BW) q.v[q.n++] = OP_W + BW; }
        for (int u = 0; u < NT; ++u) if (x1_step(u) == s) q.v[q.n++] = OP_X + u;
    }
    q.v[q.n++] = MK_T + G1 - 1;
    for (int t = 0; t < 3; ++t) q.v[q.n++] = OP_ST + t;
    for (int t = G1; t < NT; ++t) {
        for (int u = 0; u < NT; ++u) if (x2_tile(u) == t) q.v[q.n++] = OP_X + u;
        q.v[q.n++] = MK_T + t;
        if (t & 1) { q.v[q.n++] = OP_ST + t - 2; q.v[q.n++] = OP_ST + t - 1; }
    }
    return q;
}
constexpr Seq SEQ = build_seq();
constexpr int count_after(int op, int marker) {
    int pm = -1; for (int i = 0; i < SEQ.n; ++i) if (SEQ.v[i] == marker) pm = i;
    int po = -1; for (int i = 0; i < pm; ++i) if (SEQ.v[i] == op) po = i;
    if (po < 0) return -1;
    int n = 0; for (int i = po + 1; i < pm; ++i) if (SEQ.v[i] < MK_W) ++n;
    return n;
}
constexpr bool sched_ok() {
    for (int u = RING; u < NT; ++u) {
        int v = u - RING, freed = v < G1 ? G1 - 1 : (v | 1);
        if (x2_tile(u) <= freed) return false;
    }
    return true;
}
static_assert(sched_ok(), "bad DMA schedule");

template <int T, int S, int SLOT> __device__ __forceinline__ void frag_read(Ctx& c) {
    constexpr int off = (T % RING) * TILE_B + 256 * (S >> 2);
    constexpr int hi = off & ~0xFFFF, lo = off & 0xFFFF;
    lds_rd<lo>(c.xf[SLOT], c.rd[S & 3] + hi);
}
template <int A> __device__ __forceinline__ u32x4 pack_out(Ctx& c) {
    u32x4 o;
#pragma unroll
    for (int r = 0; r < 4; ++r) {
        o[r] = cvt_pk(c.acc[A][0][r], c.acc[A][1][r]);
    }
    return o;
}
template <int S> __device__ __forceinline__ void wload(Ctx& c) {
    const char* p = c.wp + (size_t)(32 * S) * ROWB;
    gload1<0 * ROWB>(c.d[S][0], p); gload1<1 * ROWB>(c.d[S][1], p); gload1<2 * ROWB>(c.d[S][2], p);
    gload1<3 * ROWB>(c.d[S][3], p); gload1<4 * ROWB>(c.d[S][4], p);
    const char* p2 = p + 5 * ROWB;
    gload1<0 * ROWB>(c.d[S][5], p2); gload1<1 * ROWB>(c.d[S][6], p2); gload1<2 * ROWB>(c.d[S][7], p2);
    if constexpr (S == BW) asm volatile("global_load_dwordx4 %0, %1, off" : "=v"(c.bb) : "v"(c.bp));
}
template <int S, int E> __device__ __forceinline__ void wload_range(Ctx& c) {
    if constexpr (S < E) { wload<S>(c); wload_range<S + 1, E>(c); }
}

template <int S, int TT> __device__ __forceinline__ void p1_tile(Ctx& c) {
    if constexpr (TT < G1) {
        constexpr int g = G1 * S + TT, A = (TT + 2) & 3;
        wait_lgkm<cmin(2, 12 * G1 - 1 - g)>(c.xf[g % 4]);
        const bf16x8 xv = __builtin_bit_cast(bf16x8, c.xf[g % 4]);
#pragma unroll
        for (int i = 0; i < 2; ++i) {
            if constexpr (S == 0) c.acc[A][i] = __builtin_amdgcn_mfma_f32_16x16x32_bf16(c.wf[i][S], xv, c.binit[i], 0, 0, 0);
            else c.acc[A][i] = __builtin_amdgcn_mfma_f32_16x16x32_bf16(c.wf[i][S], xv, c.acc[A][i], 0, 0, 0);
        }
        __builtin_amdgcn_sched_barrier(0);
        if constexpr (g + 3 < 12 * G1) frag_read<(g + 3) % G1, (g + 3) / G1, (g + 3) % 4>(c);
        p1_tile<S, TT + 1>(c);
    }
}
template <int S> __device__ __forceinline__ void p1_step(Ctx& c) {
    if constexpr (S < 12) {
        constexpr int N = count_after(OP_W + S, MK_W + S);
        if constexpr (S == BW)
            asm volatile("s_waitcnt vmcnt(%9)" : "+v"(c.d[S][0]), "+v"(c.d[S][1]), "+v"(c.d[S][2]), "+v"(c.d[S][3]), "+v"(c.d[S][4]),
                         "+v"(c.d[S][5]), "+v"(c.d[S][6]), "+v"(c.d[S][7]), "+v"(c.bb) : "n"(N) : "memory");
        else
            asm volatile("s_waitcnt vmcnt(%8)" : "+v"(c.d[S][0]), "+v"(c.d[S][1]), "+v"(c.d[S][2]), "+v"(c.d[S][3]), "+v"(c.d[S][4]),
                         "+v"(c.d[S][5]), "+v"(c.d[S][6]), "+v"(c.d[S][7]) : "n"(N) : "memory");
        if constexpr (S == 0) {
            __builtin_amdgcn_s_barrier();
            frag_read<0, 0, 0>(c); frag_read<1, 0, 1>(c); frag_read<2, 0, 2>(c);
#pragma unroll
            for (int e = 0; e < 4; ++e) {
                c.binit[0][e] = __builtin_bit_cast(float, c.bb[e] << 16);
                c.binit[1][e] = __builtin_bit_cast(float, c.bb[e] & 0xffff0000u);
            }
        }
        if constexpr (WI + WPS * S < 12) wload_range<WI + WPS * S, cmin(12, WI + WPS * (S + 1))>(c);
        if constexpr (S >= P1O && (S - P1O) % P1S == 0 && (S - P1O) / P1S < P1N) dma_tile<G1 + (S - P1O) / P1S>(c);
        u32x4 f0, f1;
#pragma unroll
        for (int p = 0; p < 4; ++p) {
            f0[p] = __builtin_amdgcn_perm(c.d[S][2 * p + 1], c.d[S][2 * p], 0x05040100u);
            f1[p] = __builtin_amdgcn_perm(c.d[S][2 * p + 1], c.d[S][2 * p], 0x07060302u);
        }
        c.wf[0][S] = __builtin_bit_cast(bf16x8, f0);
        c.wf[1][S] = __builtin_bit_cast(bf16x8, f1);
        p1_tile<S, 0>(c);
        p1_step<S + 1>(c);
    }
}

struct Loop {
    unsigned ra[4];
    unsigned rn[3];
    const char* xs;
    unsigned xd;
    char* op;
    unsigned ob[2];
};
template <int P> __device__ __forceinline__ void pair_wait(int p) {
    if constexpr (P < NPAIR - 1) {
        if (p == P) wait_vm<count_after(OP_X + G1 + 2 * P + 3, MK_T + G1 + 2 * P + 1)>();
        else pair_wait<P + 1>(p);
    }
}
constexpr int fw(int H, int S) { return 2 + ((S >= 1 && S <= 3) ? 1 : 0) + (((H == 1 && S >= 10) || (H == 0 && S == 0)) ? 2 : 0); }
template <int H, int S> __device__ __forceinline__ void kstep_rt(Ctx& c, Loop& L, int p) {
    if constexpr (S < 12) {
        wait_lgkm<fw(H, S)>(c.xf[S % 4]);
        const bf16x8 xv = __builtin_bit_cast(bf16x8, c.xf[S % 4]);
        u32x4 o;
        if constexpr (S == 0) o = pack_out<1 - H>(c);
#pragma unroll
        for (int i = 0; i < 2; ++i) {
            if constexpr (S == 0) c.acc[H][i] = __builtin_amdgcn_mfma_f32_16x16x32_bf16(c.wf[i][S], xv, c.binit[i], 0, 0, 0);
            else c.acc[H][i] = __builtin_amdgcn_mfma_f32_16x16x32_bf16(c.wf[i][S], xv, c.acc[H][i], 0, 0, 0);
        }
        __builtin_amdgcn_sched_barrier(0);
        if constexpr (S == 9 && H == 1) {
            pair_wait<0>(p);
            asm volatile("s_waitcnt lgkmcnt(0)" ::: "memory");
            __builtin_amdgcn_s_barrier();
        }
        if constexpr (S == 11 && H == 1) {
            asm volatile("s_waitcnt lgkmcnt(1)" : "+v"(c.ov[0]), "+v"(c.ov[1]));
            *(u32x4*)L.op = c.ov[0];
            *(u32x4*)(L.op + TSTRIDE) = c.ov[1];
            L.op += 2 * TSTRIDE;
        }
        if constexpr (S + 3 < 12) lds_rd<H * TILE_B + 256 * ((S + 3) >> 2)>(c.xf[(S + 3) % 4], L.ra[(S + 3) & 3]);
        else if constexpr (H == 0) lds_rd<TILE_B>(c.xf[(S + 3) % 4], L.ra[S - 9]);
        else lds_rd<0>(c.xf[(S + 3) % 4], L.rn[S - 9]);
        if constexpr (S == 0) lds_wr<0>(c.owr + L.ob[H], o);
        if constexpr (S == 9 && H == 1) { lds_rd<0>(c.ov[0], c.ord + L.ob[0]); lds_rd<0>(c.ov[1], c.ord + L.ob[1]); }
        if constexpr (S == 1) {
            if (G1 + P1N + 2 * p + H < NT) {
                dma_rt(L.xs, L.xd);
                L.xs += TSTRIDE;
                L.xd += TILE_B;
                if (L.xd >= c.dmadst + RING * TILE_B) L.xd -= RING * TILE_B;
            }
        }
        kstep_rt<H, S + 1>(c, L, p);
    }
}

__global__ __launch_bounds__(768, 3) void conv1x1_kernel(const char* __restrict__ x, const char* __restrict__ w,
                                                         const char* __restrict__ b, char* __restrict__ out) {
    __shared__ __attribute__((aligned(16))) char lds[LDS_B];
    const int tid = threadIdx.x;
    const int wv = __builtin_amdgcn_readfirstlane(tid >> 6);
    const int lane = tid & 63;
    const int r16 = lane & 15, q = lane >> 4;
    const size_t row0 = (size_t)blockIdx.x * BM;

    Ctx c;
    const unsigned lbase = (unsigned)(size_t)(__attribute__((address_space(3))) char*)lds;
    {
        const int S = wv * 64 + lane;
        const int row = S / 48, cs = S % 48;
        const int chunk = (cs & ~15) | ((cs ^ row) & 15);
        c.xsrc = x + (row0 + row) * ROWB + chunk * 16;
        c.ord = lbase + OB0 + row * OROW + cs * 16;
    }
    c.dmadst = lbase + wv * 1024;
    dma_range<0, G1>(c);
    c.wp = w + (size_t)(8 * q) * ROWB + (32 * wv + 2 * r16) * 2;
    c.bp = b + (32 * wv + 8 * q) * 2;
    wload_range<0, WI>(c);
#pragma unroll
    for (int v = 0; v < 4; ++v) c.rd[v] = lbase + r16 * ROWB + 16 * ((q ^ r16) ^ (4 * v));
    c.owr = lbase + OB0 + r16 * OROW + 64 * wv + 16 * q;
    c.ost = out + row0 * ROWB + wv * 1024 + lane * 16;
    p1_step<0>(c);
    lds_wr<0>(c.owr, pack_out<2>(c)); lds_wr<OBUF_B>(c.owr, pack_out<3>(c)); lds_wr<2 * OBUF_B>(c.owr, pack_out<0>(c));
    wait_vm<count_after(OP_X + G1 + 1, MK_T + G1 - 1)>();
    asm volatile("s_waitcnt lgkmcnt(0)" ::: "memory");
    __builtin_amdgcn_s_barrier();
    frag_read<G1, 0, 0>(c); frag_read<G1, 1, 1>(c); frag_read<G1, 2, 2>(c);
    lds_rd<0>(c.ov[0], c.ord); lds_rd<OBUF_B>(c.ov[1], c.ord); lds_rd<2 * OBUF_B>(c.ov[2], c.ord);
    asm volatile("s_waitcnt lgkmcnt(0)" : "+v"(c.ov[0]), "+v"(c.ov[1]), "+v"(c.ov[2]));
    *(u32x4*)c.ost = c.ov[0]; *(u32x4*)(c.ost + TSTRIDE) = c.ov[1]; *(u32x4*)(c.ost + 2 * TSTRIDE) = c.ov[2];
    __builtin_amdgcn_sched_barrier(0);
    Loop L;
    L.xs = c.xsrc + (size_t)(G1 + P1N) * TSTRIDE;
    L.xd = c.dmadst + ((G1 + P1N) % RING) * TILE_B;
    L.op = c.ost + (size_t)3 * TSTRIDE;
    unsigned sb = G1 * TILE_B;
    unsigned ob = 3 * OBUF_B;
#pragma unroll 1
    for (int p = 0; p < NPAIR; ++p) {
        unsigned nb = sb + 2 * TILE_B;
        if (nb >= RING * TILE_B) nb = 0;
        L.ob[0] = ob;
        L.ob[1] = ob + OBUF_B >= NOB * OBUF_B ? 0 : ob + OBUF_B;
        ob = L.ob[1] + OBUF_B >= NOB * OBUF_B ? 0 : L.ob[1] + OBUF_B;
#pragma unroll
        for (int v = 0; v < 4; ++v) L.ra[v] = c.rd[v] + sb;
#pragma unroll
        for (int v = 0; v < 3; ++v) L.rn[v] = c.rd[v] + nb;
        kstep_rt<0, 0>(c, L, p);
        kstep_rt<1, 0>(c, L, p);
        sb = nb;
    }
    lds_wr<0>(c.owr + ob, pack_out<1>(c));
    asm volatile("s_waitcnt lgkmcnt(0)" ::: "memory");
    __builtin_amdgcn_s_barrier();
    lds_rd<0>(c.ov[0], c.ord + ob);
    asm volatile("s_waitcnt lgkmcnt(0)" : "+v"(c.ov[0]));
    *(u32x4*)L.op = c.ov[0];
}

__global__ void conv1x1_generic(const __hip_bfloat16* __restrict__ x, const __hip_bfloat16* __restrict__ w,
                                const __hip_bfloat16* __restrict__ b, __hip_bfloat16* __restrict__ out, long M, int C) {
    const long idx = (long)blockIdx.x * blockDim.x + threadIdx.x;
    if (idx >= M * C) return;
    const long m = idx / C;
    const int n = (int)(idx % C);
    float acc = 0.f;
    for (int k = 0; k < C; ++k) acc += __bfloat162float(x[m * C + k]) * __bfloat162float(w[(long)k * C + n]);
    out[idx] = __float2bfloat16(acc + __bfloat162float(b[n]));
}

extern "C" void kernel_launch(void* const* d_in, const int* in_sizes, int n_in, void* d_out, int out_size, void* d_ws,
                              size_t ws_size, hipStream_t stream) {
    const int C = in_sizes[2];
    const long M = (long)in_sizes[0] / C;
    if (C == CDIM && M == (long)NWG * NT * BM && in_sizes[1] == CDIM * CDIM) {
        conv1x1_kernel<<<NWG, 768, 0, stream>>>((const char*)d_in[0], (const char*)d_in[1], (const char*)d_in[2], (char*)d_out);
    } else {
        const long n = M * C;
        conv1x1_generic<<<(unsigned)((n + 255) / 256), 256, 0, stream>>>((const __hip_bfloat16*)d_in[0], (const __hip_bfloat16*)d_in[1],
                                                                         (const __hip_bfloat16*)d_in[2], (__hip_bfloat16*)d_out, M, C);
    }
}
```

```cpp
#include <hip/hip_runtime.h>
#include <hip/hip_bf16.h>
#include <stdint.h>


typedef __attribute__((ext_vector_type(8))) __bf16 bf16x8;
typedef __attribute__((ext_vector_type(2))) __bf16 bf16x2;
typedef __attribute__((ext_vector_type(4))) float f32x4;
typedef __attribute__((ext_vector_type(2))) float f32x2;
typedef __attribute__((ext_vector_type(4))) unsigned u32x4;

constexpr int CDIM = 384;
constexpr int ROWB = CDIM * 2;
constexpr int BM = 16;
constexpr int TILE_B = BM * ROWB;
constexpr int NWAVE = 12;
constexpr int RING = 8;
constexpr int NOB = 5;
constexpr int OROW = ROWB + 16;
constexpr int OBUF_B = BM * OROW;
constexpr int OB0 = RING * TILE_B;
constexpr int LDS_B = OB0 + NOB * OBUF_B;
constexpr int NWG = 256;
constexpr int NT = 16;
constexpr size_t TSTRIDE = (size_t)NWG * TILE_B;
constexpr int G1 = 4;
constexpr int WI = 6;
constexpr int WPS = 1;
constexpr int P1N = 4;
constexpr int P1S = 1;
constexpr int NPAIR = (NT - G1) / 2;
constexpr int BW = 0;

__device__ __forceinline__ unsigned cvt_pk(float a, float b) {
    bf16x2 r = __builtin_convertvector((f32x2){a, b}, bf16x2);
    return __builtin_bit_cast(unsigned, r);
}

struct Ctx {
    bf16x8 wf[2][12];
    unsigned d[12][8];
    u32x4 bb;
    f32x4 binit[2];
    f32x4 acc[G1][2];
    u32x4 xf[4];
    u32x4 ov[3];
    const char* xsrc;
    const char* wp;
    const char* bp;
    char* ost;
    unsigned rd[4];
    unsigned owr, ord;
    unsigned dmadst;
};

__device__ __forceinline__ void st16(char* p, u32x4 o) {
    asm volatile("global_store_dwordx4 %0, %1, off sc1\n\ts_nop 1" ::"v"(p), "v"(o) : "memory");
}
template <int N> __device__ __forceinline__ void wait_vm() {
    static_assert(N >= 0 && N < 64, "vmcnt range");
    asm volatile("s_waitcnt vmcnt(%0)" ::"n"(N) : "memory");
}
__device__ __forceinline__ void dma_rt(const char* g, unsigned dst) {
    unsigned keep;
    asm volatile("s_mov_b32 %0, m0\n\ts_mov_b32 m0, %2\n\ts_nop 0\n\tglobal_load_lds_dwordx4 %1, off\n\ts_mov_b32 m0, %0"
                 : "=&s"(keep) : "v"(g), "s"(dst) : "memory");
}
template <int T> __device__ __forceinline__ void dma_tile(const Ctx& c) {
    dma_rt(c.xsrc + (size_t)T * TSTRIDE, c.dmadst + (T % RING) * TILE_B);
}
template <int T, int E> __device__ __forceinline__ void dma_range(const Ctx& c) {
    if constexpr (T < E) { dma_tile<T>(c); dma_range<T + 1, E>(c); }
}
template <int OFF> __device__ __forceinline__ void lds_rd(u32x4& d, unsigned addr) {
    asm volatile("ds_read_b128 %0, %1 offset:%2" : "=v"(d) : "v"(addr), "n"(OFF));
}
template <int OFF> __device__ __forceinline__ void lds_wr(unsigned addr, u32x4 d) {
    asm volatile("ds_write_b128 %0, %1 offset:%2" ::"v"(addr), "v"(d), "n"(OFF) : "memory");
}
template <int N> __device__ __forceinline__ void wait_lgkm(u32x4& d) {
    asm volatile("s_waitcnt lgkmcnt(%1)" : "+v"(d) : "n"(N));
}
template <int OFF> __device__ __forceinline__ void gload1(unsigned& d, const char* p) {
    asm volatile("global_load_dword %0, %1, off offset:%2" : "=v"(d) : "v"(p), "n"(OFF));
}

constexpr int cmin(int a, int b) { return a < b ? a : b; }
constexpr int w_step(int s) { return s < WI ? -1 : (s - WI) / WPS; }
constexpr int P1O = 4;
constexpr int x1_step(int u) { return (u >= G1 && u < G1 + P1N) ? P1O + P1S * (u - G1) : -1; }
constexpr int x2_tile(int u) { return u >= G1 + P1N ? u - P1N : -1; }
enum { OP_X = 1000, OP_W = 2000, OP_ST = 3000, MK_W = 4000, MK_T = 5000 };
struct Seq { int v[600]; int n; };
constexpr Seq build_seq() {
    Seq q{}; q.n = 0;
    for (int u = 0; u < G1; ++u) q.v[q.n++] = OP_X + u;
    for (int s = 0; s < WI; ++s) { for (int j = 0; j < 8; ++j) q.v[q.n++] = OP_W + s; if (s == BW) q.v[q.n++] = OP_W + s; }
    for (int s = 0; s < 12; ++s) {
        q.v[q.n++] = MK_W + s;
        for (int v = 0; v < 12; ++v) if (w_step(v) == s) { for (int j = 0; j < 8; ++j) q.v[q.n++] = OP_W + v; if (v == BW) q.v[q.n++] = OP_W + BW; }
        for (int u = 0; u < NT; ++u) if (x1_step(u) == s) q.v[q.n++] = OP_X + u;
    }
    q.v[q.n++] = MK_T + G1 - 1;
    for (int t = 0; t < 3; ++t) q.v[q.n++] = OP_ST + t;
    for (int t = G1; t < NT; ++t) {
        for (int u = 0; u < NT; ++u) if (x2_tile(u) == t) q.v[q.n++] = OP_X + u;
        q.v[q.n++] = MK_T + t;
        if (t & 1) { q.v[q.n++] = OP_ST + t - 2; q.v[q.n++] = OP_ST + t - 1; }
    }
    return q;
}
constexpr Seq SEQ = build_seq();
constexpr int count_after(int op, int marker) {
    int pm = -1; for (int i = 0; i < SEQ.n; ++i) if (SEQ.v[i] == marker) pm = i;
    int po = -1; for (int i = 0; i < pm; ++i) if (SEQ.v[i] == op) po = i;
    if (po < 0) return -1;
    int n = 0; for (int i = po + 1; i < pm; ++i) if (SEQ.v[i] < MK_W) ++n;
    return n;
}
constexpr bool sched_ok() {
    for (int u = RING; u < NT; ++u) {
        int v = u - RING, freed = v < G1 ? G1 - 1 : (v | 1);
        if (x2_tile(u) <= freed) return false;
    }
    return true;
}
static_assert(sched_ok(), "bad DMA schedule");

template <int T, int S, int SLOT> __device__ __forceinline__ void frag_read(Ctx& c) {
    constexpr int off = (T % RING) * TILE_B + 256 * (S >> 2);
    constexpr int hi = off & ~0xFFFF, lo = off & 0xFFFF;
    lds_rd<lo>(c.xf[SLOT], c.rd[S & 3] + hi);
}
template <int A> __device__ __forceinline__ u32x4 pack_out(Ctx& c) {
    u32x4 o;
#pragma unroll
    for (int r = 0; r < 4; ++r) {
        o[r] = cvt_pk(c.acc[A][0][r], c.acc[A][1][r]);
    }
    return o;
}
template <int S> __device__ __forceinline__ void wload(Ctx& c) {
    const char* p = c.wp + (size_t)(32 * S) * ROWB;
    gload1<0 * ROWB>(c.d[S][0], p); gload1<1 * ROWB>(c.d[S][1], p); gload1<2 * ROWB>(c.d[S][2], p);
    gload1<3 * ROWB>(c.d[S][3], p); gload1<4 * ROWB>(c.d[S][4], p);
    const char* p2 = p + 5 * ROWB;
    gload1<0 * ROWB>(c.d[S][5], p2); gload1<1 * ROWB>(c.d[S][6], p2); gload1<2 * ROWB>(c.d[S][7], p2);
    if constexpr (S == BW) asm volatile("global_load_dwordx4 %0, %1, off" : "=v"(c.bb) : "v"(c.bp));
}
template <int S, int E> __device__ __forceinline__ void wload_range(Ctx& c) {
    if constexpr (S < E) { wload<S>(c); wload_range<S + 1, E>(c); }
}

template <int S, int TT> __device__ __forceinline__ void p1_tile(Ctx& c) {
    if constexpr (TT < G1) {
        constexpr int g = G1 * S + TT, A = (TT + 2) & 3;
        wait_lgkm<cmin(2, 12 * G1 - 1 - g)>(c.xf[g % 4]);
        const bf16x8 xv = __builtin_bit_cast(bf16x8, c.xf[g % 4]);
#pragma unroll
        for (int i = 0; i < 2; ++i) {
            if constexpr (S == 0) c.acc[A][i] = __builtin_amdgcn_mfma_f32_16x16x32_bf16(c.wf[i][S], xv, c.binit[i], 0, 0, 0);
            else c.acc[A][i] = __builtin_amdgcn_mfma_f32_16x16x32_bf16(c.wf[i][S], xv, c.acc[A][i], 0, 0, 0);
        }
        __builtin_amdgcn_sched_barrier(0);
        if constexpr (g + 3 < 12 * G1) frag_read<(g + 3) % G1, (g + 3) / G1, (g + 3) % 4>(c);
        p1_tile<S, TT + 1>(c);
    }
}
template <int S> __device__ __forceinline__ void p1_step(Ctx& c) {
    if constexpr (S < 12) {
        constexpr int N = count_after(OP_W + S, MK_W + S);
        if constexpr (S == BW)
            asm volatile("s_waitcnt vmcnt(%9)" : "+v"(c.d[S][0]), "+v"(c.d[S][1]), "+v"(c.d[S][2]), "+v"(c.d[S][3]), "+v"(c.d[S][4]),
                         "+v"(c.d[S][5]), "+v"(c.d[S][6]), "+v"(c.d[S][7]), "+v"(c.bb) : "n"(N) : "memory");
        else
            asm volatile("s_waitcnt vmcnt(%8)" : "+v"(c.d[S][0]), "+v"(c.d[S][1]), "+v"(c.d[S][2]), "+v"(c.d[S][3]), "+v"(c.d[S][4]),
                         "+v"(c.d[S][5]), "+v"(c.d[S][6]), "+v"(c.d[S][7]) : "n"(N) : "memory");
        if constexpr (S == 0) {
            __builtin_amdgcn_s_barrier();
            frag_read<0, 0, 0>(c); frag_read<1, 0, 1>(c); frag_read<2, 0, 2>(c);
#pragma unroll
            for (int e = 0; e < 4; ++e) {
                c.binit[0][e] = __builtin_bit_cast(float, c.bb[e] << 16);
                c.binit[1][e] = __builtin_bit_cast(float, c.bb[e] & 0xffff0000u);
            }
        }
        if constexpr (WI + WPS * S < 12) wload_range<WI + WPS * S, cmin(12, WI + WPS * (S + 1))>(c);
        if constexpr (S >= P1O && (S - P1O) % P1S == 0 && (S - P1O) / P1S < P1N) dma_tile<G1 + (S - P1O) / P1S>(c);
        u32x4 f0, f1;
#pragma unroll
        for (int p = 0; p < 4; ++p) {
            f0[p] = __builtin_amdgcn_perm(c.d[S][2 * p + 1], c.d[S][2 * p], 0x05040100u);
            f1[p] = __builtin_amdgcn_perm(c.d[S][2 * p + 1], c.d[S][2 * p], 0x07060302u);
        }
        c.wf[0][S] = __builtin_bit_cast(bf16x8, f0);
        c.wf[1][S] = __builtin_bit_cast(bf16x8, f1);
        p1_tile<S, 0>(c);
        p1_step<S + 1>(c);
    }
}

struct Loop {
    unsigned ra[4];
    unsigned rn[3];
    const char* xs;
    unsigned xd;
    char* op;
    unsigned ob[2];
};
template <int P> __device__ __forceinline__ void pair_wait(int p) {
    if constexpr (P < NPAIR - 1) {
        if (p == P) wait_vm<count_after(OP_X + G1 + 2 * P + 3, MK_T + G1 + 2 * P + 1)>();
        else pair_wait<P + 1>(p);
    }
}
constexpr int fw(int H, int S) { return 2 + ((S >= 1 && S <= 3) ? 1 : 0) + (((H == 1 && S >= 10) || (H == 0 && S == 0)) ? 2 : 0); }
template <int H, int S> __device__ __forceinline__ void kstep_rt(Ctx& c, Loop& L, int p) {
    if constexpr (S < 12) {
        wait_lgkm<fw(H, S)>(c.xf[S % 4]);
        const bf16x8 xv = __builtin_bit_cast(bf16x8, c.xf[S % 4]);
        u32x4 o;
        if constexpr (S == 0) o = pack_out<1 - H>(c);
#pragma unroll
        for (int i = 0; i < 2; ++i) {
            if constexpr (S == 0) c.acc[H][i] = __builtin_amdgcn_mfma_f32_16x16x32_bf16(c.wf[i][S], xv, c.binit[i], 0, 0, 0);
            else c.acc[H][i] = __builtin_amdgcn_mfma_f32_16x16x32_bf16(c.wf[i][S], xv, c.acc[H][i], 0, 0, 0);
        }
        __builtin_amdgcn_sched_barrier(0);
        if constexpr (S == 9 && H == 1) {
            pair_wait<0>(p);
            asm volatile("s_waitcnt lgkmcnt(0)" ::: "memory");
            __builtin_amdgcn_s_barrier();
        }
        if constexpr (S == 11 && H == 1) {
            asm volatile("s_waitcnt lgkmcnt(1)" : "+v"(c.ov[0]), "+v"(c.ov[1]));
            st16(L.op, c.ov[0]);
            st16(L.op + TSTRIDE, c.ov[1]);
            L.op += 2 * TSTRIDE;
        }
        if constexpr (S + 3 < 12) lds_rd<H * TILE_B + 256 * ((S + 3) >> 2)>(c.xf[(S + 3) % 4], L.ra[(S + 3) & 3]);
        else if constexpr (H == 0) lds_rd<TILE_B>(c.xf[(S + 3) % 4], L.ra[S - 9]);
        else lds_rd<0>(c.xf[(S + 3) % 4], L.rn[S - 9]);
        if constexpr (S == 0) lds_wr<0>(c.owr + L.ob[H], o);
        if constexpr (S == 9 && H == 1) { lds_rd<0>(c.ov[0], c.ord + L.ob[0]); lds_rd<0>(c.ov[1], c.ord + L.ob[1]); }
        if constexpr (S == 1) {
            if (G1 + P1N + 2 * p + H < NT) {
                dma_rt(L.xs, L.xd);
                L.xs += TSTRIDE;
                L.xd += TILE_B;
                if (L.xd >= c.dmadst + RING * TILE_B) L.xd -= RING * TILE_B;
            }
        }
        kstep_rt<H, S + 1>(c, L, p);
    }
}

__global__ __launch_bounds__(768, 3) void conv1x1_kernel(const char* __restrict__ x, const char* __restrict__ w,
                                                         const char* __restrict__ b, char* __restrict__ out) {
    __shared__ __attribute__((aligned(16))) char lds[LDS_B];
    const int tid = threadIdx.x;
    const int wv = __builtin_amdgcn_readfirstlane(tid >> 6);
    const int lane = tid & 63;
    const int r16 = lane & 15, q = lane >> 4;
    const size_t row0 = (size_t)blockIdx.x * BM;

    Ctx c;
    const unsigned lbase = (unsigned)(size_t)(__attribute__((address_space(3))) char*)lds;
    {
        const int S = wv * 64 + lane;
        const int row = S / 48, cs = S % 48;
        const int chunk = (cs & ~15) | ((cs ^ row) & 15);
        c.xsrc = x + (row0 + row) * ROWB + chunk * 16;
        c.ord = lbase + OB0 + row * OROW + cs * 16;
    }
    c.dmadst = lbase + wv * 1024;
    dma_range<0, G1>(c);
    c.wp = w + (size_t)(8 * q) * ROWB + (32 * wv + 2 * r16) * 2;
    c.bp = b + (32 * wv + 8 * q) * 2;
    wload_range<0, WI>(c);
#pragma unroll
    for (int v = 0; v < 4; ++v) c.rd[v] = lbase + r16 * ROWB + 16 * ((q ^ r16) ^ (4 * v));
    c.owr = lbase + OB0 + r16 * OROW + 64 * wv + 16 * q;
    c.ost = out + row0 * ROWB + wv * 1024 + lane * 16;
    p1_step<0>(c);
    lds_wr<0>(c.owr, pack_out<2>(c)); lds_wr<OBUF_B>(c.owr, pack_out<3>(c)); lds_wr<2 * OBUF_B>(c.owr, pack_out<0>(c));
    wait_vm<count_after(OP_X + G1 + 1, MK_T + G1 - 1)>();
    asm volatile("s_waitcnt lgkmcnt(0)" ::: "memory");
    __builtin_amdgcn_s_barrier();
    frag_read<G1, 0, 0>(c); frag_read<G1, 1, 1>(c); frag_read<G1, 2, 2>(c);
    lds_rd<0>(c.ov[0], c.ord); lds_rd<OBUF_B>(c.ov[1], c.ord); lds_rd<2 * OBUF_B>(c.ov[2], c.ord);
    asm volatile("s_waitcnt lgkmcnt(0)" : "+v"(c.ov[0]), "+v"(c.ov[1]), "+v"(c.ov[2]));
    st16(c.ost, c.ov[0]); st16(c.ost + TSTRIDE, c.ov[1]); st16(c.ost + 2 * TSTRIDE, c.ov[2]);
    __builtin_amdgcn_sched_barrier(0);
    Loop L;
    L.xs = c.xsrc + (size_t)(G1 + P1N) * TSTRIDE;
    L.xd = c.dmadst + ((G1 + P1N) % RING) * TILE_B;
    L.op = c.ost + (size_t)3 * TSTRIDE;
    unsigned sb = G1 * TILE_B;
    unsigned ob = 3 * OBUF_B;
#pragma unroll 1
    for (int p = 0; p < NPAIR; ++p) {
        unsigned nb = sb + 2 * TILE_B;
        if (nb >= RING * TILE_B) nb = 0;
        L.ob[0] = ob;
        L.ob[1] = ob + OBUF_B >= NOB * OBUF_B ? 0 : ob + OBUF_B;
        ob = L.ob[1] + OBUF_B >= NOB * OBUF_B ? 0 : L.ob[1] + OBUF_B;
#pragma unroll
        for (int v = 0; v < 4; ++v) L.ra[v] = c.rd[v] + sb;
#pragma unroll
        for (int v = 0; v < 3; ++v) L.rn[v] = c.rd[v] + nb;
        kstep_rt<0, 0>(c, L, p);
        kstep_rt<1, 0>(c, L, p);
        sb = nb;
    }
    lds_wr<0>(c.owr + ob, pack_out<1>(c));
    asm volatile("s_waitcnt lgkmcnt(0)" ::: "memory");
    __builtin_amdgcn_s_barrier();
    lds_rd<0>(c.ov[0], c.ord + ob);
    asm volatile("s_waitcnt lgkmcnt(0)" : "+v"(c.ov[0]));
    st16(L.op, c.ov[0]);
}

__global__ void conv1x1_generic(const __hip_bfloat16* __restrict__ x, const __hip_bfloat16* __restrict__ w,
                                const __hip_bfloat16* __restrict__ b, __hip_bfloat16* __restrict__ out, long M, int C) {
    const long idx = (long)blockIdx.x * blockDim.x + threadIdx.x;
    if (idx >= M * C) return;
    const long m = idx / C;
    const int n = (int)(idx % C);
    float acc = 0.f;
    for (int k = 0; k < C; ++k) acc += __bfloat162float(x[m * C + k]) * __bfloat162float(w[(long)k * C + n]);
    out[idx] = __float2bfloat16(acc + __bfloat162float(b[n]));
}

extern "C" void kernel_launch(void* const* d_in, const int* in_sizes, int n_in, void* d_out, int out_size, void* d_ws,
                              size_t ws_size, hipStream_t stream) {
    const int C = in_sizes[2];
    const long M = (long)in_sizes[0] / C;
    if (C == CDIM && M == (long)NWG * NT * BM && in_sizes[1] == CDIM * CDIM) {
        conv1x1_kernel<<<NWG, 768, 0, stream>>>((const char*)d_in[0], (const char*)d_in[1], (const char*)d_in[2], (char*)d_out);
    } else {
        const long n = M * C;
        conv1x1_generic<<<(unsigned)((n + 255) / 256), 256, 0, stream>>>((const __hip_bfloat16*)d_in[0], (const __hip_bfloat16*)d_in[1],
                                                                         (const __hip_bfloat16*)d_in[2], (__hip_bfloat16*)d_out, M, C);
    }
}
```
